```python
import torch

def _softmax(x, axis=-1):
    return torch.softmax(x, dim=axis)
def _gelu(x):
    return torch.nn.functional.gelu(x, approximate="tanh")
def _silu(x):
    return torch.nn.functional.silu(x)
def _sigmoid(x):
    return torch.sigmoid(x)
def _layernorm(x, axis=-1, eps=1e-5):
    m = x.mean(dim=axis, keepdim=True)
    v = x.var(dim=axis, keepdim=True, unbiased=False)
    return (x - m) / torch.sqrt(v + eps)
def _scatter_add(tgt, idx, src, axis=0):
    return tgt.clone().index_add_(axis, idx, src)
def _segment_sum(src, seg, n):
    out = torch.zeros((n,) + src.shape[1:], dtype=src.dtype)
    return out.index_add_(0, seg, src)
def _pad(x, pw):
    flat = tuple(v for pair in reversed(pw) for v in pair)
    return torch.nn.functional.pad(x, flat)

kernel_name = 'parallel_sum__sliding_block_cached__dense_activation_silu__decode'

def setup_inputs():

    _w = (lambda a: (lambda t: t.to(torch.bfloat16) if t.is_floating_point() else t)(torch.as_tensor(a)))
    def _A_setup():
        B, Sq, D = 1, 1, 512
        Hq, Hkv, Sk, Dh = 32, 8, 65536, 128
        assert Hq % Hkv == 0, (Hq, Hkv)
        return {
            "x": _w((lambda *s: torch.randn(*s, dtype=torch.bfloat16) * 0.05)(B, Sq, D)),
            "k_cache": _w((lambda *s: torch.randn(*s, dtype=torch.bfloat16) * 3.2)(B, Hkv, Sk, Dh)),
            "v_cache": _w((lambda *s: torch.randn(*s, dtype=torch.bfloat16) * 0.8)(B, Hkv, Sk, Dh)),
            "wq": _w((lambda *s: torch.randn(*s, dtype=torch.bfloat16) * 3.2)(D, Hq * Dh) * (D ** -0.5)),
            "wo": _w((lambda *s: torch.randn(*s, dtype=torch.bfloat16) * 0.8)(Hq * Dh, D) * ((Hq * Dh) ** -0.5)),
        }
    _A_in = _A_setup()

    def _B_setup():
        B, S, D, F = 1, 1, 512, 1792
        return {
            "x": _w((lambda *s: torch.randn(*s, dtype=torch.bfloat16) * 0.05)(B, S, D)),
            "w1": _w((lambda *s: torch.randn(*s, dtype=torch.bfloat16) * 3.2)(D, F) * (D ** -0.5)),
            "w2": _w((lambda *s: torch.randn(*s, dtype=torch.bfloat16) * 0.4)(F, D) * (F ** -0.5)),
        }
    _B_in = _B_setup()

    return {
        "A_x": _A_in["x"],
        "A_k_cache": _A_in["k_cache"],
        "A_v_cache": _A_in["v_cache"],
        "A_wq": _A_in["wq"],
        "A_wo": _A_in["wo"],
        "B_x": _B_in["x"],
        "B_w1": _B_in["w1"],
        "B_w2": _B_in["w2"],
    }

def reference(A_x, A_k_cache, A_v_cache, A_wq, A_wo, B_x, B_w1, B_w2):
    def _A_ref(x, k_cache, v_cache, wq, wo):
        B, Sq, D = x.shape
        Hq, Dh = 32, 128
        Hkv, Sk = k_cache.shape[1], k_cache.shape[2]
        W = min(4096, max(256, Sk // 4))
        rep = Hq // Hkv
        q = (lambda x, *d: x.permute(*d))((x @ wq).reshape(B, Sq, Hq, Dh), 0, 2, 1, 3)
        k = (lambda x, n, axis: torch.repeat_interleave(x, n, dim=axis))(k_cache[:, :, Sk - W:, :], rep, axis=1)
        v = (lambda x, n, axis: torch.repeat_interleave(x, n, dim=axis))(v_cache[:, :, Sk - W:, :], rep, axis=1)
        scores = torch.einsum("bhqd,bhkd->bhqk", q, k) * (Dh ** -0.5)
        p = _softmax(scores, axis=-1)
        o = torch.einsum("bhqk,bhkd->bhqd", p, v)
        return (lambda x, *d: x.permute(*d))(o, 0, 2, 1, 3).reshape(B, Sq, Hq * Dh) @ wo

    def _B_ref(x, w1, w2):
        h = torch.einsum("bsd,df->bsf", x, w1)
        h = _silu(h)
        out = torch.einsum("bsf,fd->bsd", h, w2)
        return out

    x = A_x
    xn = _layernorm(x, axis=-1)
    return x + _A_ref(xn, A_k_cache, A_v_cache, A_wq, A_wo) + _B_ref(xn, B_w1, B_w2)
```

```cpp
#include <hip/hip_runtime.h>
#include <hip/hip_bf16.h>
#include <stdint.h>

typedef unsigned short u16;
typedef unsigned v4u __attribute__((ext_vector_type(4)));
__device__ __forceinline__ float bf2f(unsigned b) { return __builtin_bit_cast(float, b << 16); }
__device__ __forceinline__ unsigned f2bf(float f) { unsigned u = __builtin_bit_cast(unsigned, f); return (u + 0x7fffu + ((u >> 16) & 1u)) >> 16; }
__device__ __forceinline__ float rbf(float f) { return bf2f(f2bf(f)); }

constexpr int D = 512, HQ = 32, HKV = 8, SK = 65536, DH = 128, W = 4096, F = 1792;
constexpr int NSPLIT = 32;

__device__ void layernorm_to_lds(const u16* x, float* xn, float* red) {
  int t = threadIdx.x;
  float a = bf2f(x[t]), b = bf2f(x[t + 256]);
  float s = a + b;
  for (int o = 32; o; o >>= 1) s += __shfl_xor(s, o);
  if ((t & 63) == 0) red[t >> 6] = s;
  __syncthreads();
  float mean = (red[0] + red[1] + red[2] + red[3]) * (1.f / 512.f);
  __syncthreads();
  float v = (a - mean) * (a - mean) + (b - mean) * (b - mean);
  for (int o = 32; o; o >>= 1) v += __shfl_xor(v, o);
  if ((t & 63) == 0) red[t >> 6] = v;
  __syncthreads();
  float var = (red[0] + red[1] + red[2] + red[3]) * (1.f / 512.f);
  float mb = rbf(mean), vb = rbf(var);
  float den = rbf(sqrtf(rbf(vb + 1e-5f)));
  xn[t] = rbf(rbf(a - mb) / den);
  xn[t + 256] = rbf(rbf(b - mb) / den);
  __syncthreads();
}

__global__ void __launch_bounds__(256) k1(const u16* x, const u16* wq, const u16* w1, float* q, float* h) {
  __shared__ float xn[512]; __shared__ float red[4]; __shared__ float part[32][64];
  layernorm_to_lds(x, xn, red);
  int b = blockIdx.x; const u16* w; int ld, c0;
  if (b < 64) { w = wq; ld = 4096; c0 = b * 64; } else { w = w1; ld = F; c0 = (b - 64) * 64; }
  int t = threadIdx.x, cg = t & 7, rg = t >> 3;
  float acc[8] = {0};
  for (int r = rg; r < 512; r += 32) {
    v4u v = *(const v4u*)(w + (size_t)r * ld + c0 + cg * 8);
    float xv = xn[r];
    for (int j = 0; j < 4; ++j) { acc[2 * j] += xv * bf2f(v[j] & 0xffff); acc[2 * j + 1] += xv * bf2f(v[j] >> 16); }
  }
  for (int j = 0; j < 8; ++j) part[rg][cg * 8 + j] = acc[j];
  __syncthreads();
  if (t < 64) {
    float s = 0; for (int r = 0; r < 32; ++r) s += part[r][t];
    s = rbf(s);
    if (b < 64) q[c0 + t] = s; else { float g = rbf(s / (1.f + __expf(-s))); h[c0 + t] = g; }
  }
}

__global__ void __launch_bounds__(256) k2(const u16* kc, const u16* vc, const float* q, float* po, float* pml) {
  __shared__ float qs[512]; __shared__ float sc[4][128]; __shared__ float ml[4][2]; __shared__ float oacc[2][4][128];
  int s = blockIdx.x, g = blockIdx.y, t = threadIdx.x;
  qs[t] = q[g * 512 + t]; qs[t + 256] = q[g * 512 + 256 + t];
  __syncthreads();
  const u16* kb = kc + ((size_t)g * SK + (SK - W) + s * 128) * DH;
  const u16* vb = vc + ((size_t)g * SK + (SK - W) + s * 128) * DH;
  {
    int key = t >> 1, half = t & 1;
    float a[4] = {0, 0, 0, 0};
    for (int c = 0; c < 8; ++c) {
      v4u v = *(const v4u*)(kb + key * DH + half * 64 + c * 8);
      for (int j = 0; j < 4; ++j) {
        float lo = bf2f(v[j] & 0xffff), hi = bf2f(v[j] >> 16);
        int d = half * 64 + c * 8 + 2 * j;
        for (int h = 0; h < 4; ++h) a[h] += lo * qs[h * 128 + d] + hi * qs[h * 128 + d + 1];
      }
    }
    for (int h = 0; h < 4; ++h) { a[h] += __shfl_xor(a[h], 1); if (!half) sc[h][key] = rbf(rbf(a[h]) * 0.08838834764831845f); }
  }
  __syncthreads();
  {
    int h = t >> 6, l = t & 63;
    float a = sc[h][l], b = sc[h][l + 64];
    float m = fmaxf(a, b);
    for (int o = 32; o; o >>= 1) m = fmaxf(m, __shfl_xor(m, o));
    a = __expf(a - m); b = __expf(b - m);
    sc[h][l] = a; sc[h][l + 64] = b;
    float su = a + b;
    for (int o = 32; o; o >>= 1) su += __shfl_xor(su, o);
    if (l == 0) { pml[((g * 4 + h) * NSPLIT + s) * 2] = m; pml[((g * 4 + h) * NSPLIT + s) * 2 + 1] = su; }
  }
  __syncthreads();
  {
    int d = t & 127, half = t >> 7;
    float a[4] = {0, 0, 0, 0};
    for (int k = half * 64; k < half * 64 + 64; ++k) {
      float v = bf2f(vb[k * DH + d]);
      for (int h = 0; h < 4; ++h) a[h] += sc[h][k] * v;
    }
    for (int h = 0; h < 4; ++h) oacc[half][h][d] = a[h];
  }
  __syncthreads();
  for (int i = t; i < 512; i += 256) { int h = i >> 7, d = i & 127; po[((g * 4 + h) * NSPLIT + s) * 128 + d] = oacc[0][h][d] + oacc[1][h][d]; }
}

__global__ void __launch_bounds__(256) k3(const float* po, const float* pml, const float* h, const u16* wo, const u16* w2, float* part) {
  __shared__ float o[128]; __shared__ float acc2[4][512];
  int b = blockIdx.x, t = threadIdx.x;
  const u16* w;
  if (b < 32) {
    if (t < 128) {
      float M = -INFINITY; for (int s = 0; s < NSPLIT; ++s) M = fmaxf(M, pml[(b * NSPLIT + s) * 2]);
      float L = 0, a = 0;
      for (int s = 0; s < NSPLIT; ++s) { float wgt = __expf(pml[(b * NSPLIT + s) * 2] - M); L += wgt * pml[(b * NSPLIT + s) * 2 + 1]; a += wgt * po[(b * NSPLIT + s) * 128 + t]; }
      o[t] = rbf(a / L);
    }
    w = wo + (size_t)b * 128 * 512;
  } else {
    if (t < 128) o[t] = h[(b - 32) * 128 + t];
    w = w2 + (size_t)(b - 32) * 128 * 512;
  }
  __syncthreads();
  int cg = t & 63, rg = t >> 6;
  float acc[8] = {0};
  for (int r = rg; r < 128; r += 4) {
    v4u v = *(const v4u*)(w + (size_t)r * 512 + cg * 8);
    float xv = o[r];
    for (int j = 0; j < 4; ++j) { acc[2 * j] += xv * bf2f(v[j] & 0xffff); acc[2 * j + 1] += xv * bf2f(v[j] >> 16); }
  }
  for (int j = 0; j < 8; ++j) acc2[rg][cg * 8 + j] = acc[j];
  __syncthreads();
  for (int i = t; i < 512; i += 256) part[b * 512 + i] = acc2[0][i] + acc2[1][i] + acc2[2][i] + acc2[3][i];
}

__global__ void __launch_bounds__(256) k4(const float* part, const u16* x, u16* out) {
  int i = blockIdx.x * 256 + threadIdx.x;
  float a = 0, f = 0;
  for (int b = 0; b < 32; ++b) a += part[b * 512 + i];
  for (int b = 32; b < 46; ++b) f += part[b * 512 + i];
  float r = rbf(bf2f(x[i]) + rbf(a));
  out[i] = (u16)f2bf(r + rbf(f));
}

extern "C" void kernel_launch(void* const* d_in, const int* in_sizes, int n_in, void* d_out, int out_size, void* d_ws, size_t ws_size, hipStream_t stream) {
  const u16* x = (const u16*)d_in[0]; const u16* kc = (const u16*)d_in[1]; const u16* vc = (const u16*)d_in[2];
  const u16* wq = (const u16*)d_in[3]; const u16* wo = (const u16*)d_in[4]; const u16* w1 = (const u16*)d_in[6]; const u16* w2 = (const u16*)d_in[7];
  float* ws = (float*)d_ws;
  float* q = ws; float* h = ws + 4096; float* pml = ws + 8192; float* po = ws + 16384; float* part = po + 32 * 32 * 128;
  k1<<<92, 256, 0, stream>>>(x, wq, w1, q, h);
  k2<<<dim3(32, 8), 256, 0, stream>>>(kc, vc, q, po, pml);
  k3<<<46, 256, 0, stream>>>(po, pml, h, wo, w2, part);
  k4<<<2, 256, 0, stream>>>(part, x, (u16*)d_out);
}
```

```cpp
#include <hip/hip_runtime.h>
#include <stdint.h>
#pragma clang diagnostic ignored "-Wint-to-pointer-cast"

typedef unsigned short u16;
typedef unsigned long long u64;
typedef unsigned v4u __attribute__((ext_vector_type(4)));
typedef unsigned v2u __attribute__((ext_vector_type(2)));
typedef float v4f __attribute__((ext_vector_type(4)));
typedef short v4s __attribute__((ext_vector_type(4)));
typedef __bf16 v8bf __attribute__((ext_vector_type(8)));
typedef __bf16 v2bf __attribute__((ext_vector_type(2)));
typedef float v2f __attribute__((ext_vector_type(2)));
#define GAS __attribute__((address_space(1)))
#define LAS __attribute__((address_space(3)))

#define LOWW 18
#define HIGHW 28
#define SCH_B -15
#define SCH_G 61
#define T_R 85
#define T_INV 70
#define T_LAG 28
#define T_H1P 212
#define T_H1M 206
#define T_FB 238
#ifndef M_H1
#define M_H1 68
#endif
#define T_H2 443
#define D_ML 6
#define T_HH 474
#define D_H3 3
#define PFH 24
#define T_H3 544
#define CODE_LINES 70

constexpr unsigned L_WQ = 0, L_K = 16384, L_V = 49152, L_W1 = 81920, L_WO = 90112  , L_XN = 114688  ,
                   L_Q = L_XN + 4096  , L_P = L_Q + 1088  , L_M = L_P + 1024  ,
                   L_A = L_M + 64  , L_FLG = L_A + 192, L_END = L_FLG + 48;
constexpr unsigned F_LOAD = L_FLG, F_Q = L_FLG + 4, F_P = L_FLG + 8, F_D = L_FLG + 16, F_T = L_FLG + 20,
                   F_R = L_FLG + 24, F_R1 = L_FLG + 28, F_A = L_FLG + 32, F_H = L_FLG + 36;
constexpr unsigned NZW = 12;
constexpr unsigned W_EPOCH = 0, W_TREF = 1024  , W_QG = 4096  , W_ML = W_QG + 65536  , W_HG = W_ML + 32768  ,
                   W_OG = 0x40000  , W_P3 = W_OG + 0x80000  , W_END = W_P3 + 0x40000;
static_assert(W_HG + 8 * 1792 * 8 <= W_OG, "ws map");
static_assert(PFH + 32 <= CODE_LINES && CODE_LINES <= PFH + 64, "prefetch shares");
constexpr unsigned SPIN_CAP = 1u << 16;
constexpr unsigned M_WQ = 16, M_K = 48, M_V2 = 72, M_V3 = 80, M_W1 = 88, M_ALL = 111;
constexpr unsigned OOB = 0x80000000u;
constexpr float LOG2E = 1.4426950408889634f;

#define NOUNROLL _Pragma("clang loop unroll(disable)")
#define FENCE() asm volatile("" ::: "memory")
#define UNLIKELY(x) __builtin_expect(!!(x), 0)

__device__ __forceinline__ float asf(unsigned u) { return __builtin_bit_cast(float, u); }
__device__ __forceinline__ unsigned asu(float f) { return __builtin_bit_cast(unsigned, f); }
__device__ __forceinline__ float bf_lo(unsigned u) { return asf(u << 16); }
__device__ __forceinline__ float bf_hi(unsigned u) { return asf(u & 0xffff0000u); }
__device__ __forceinline__ unsigned pk_bf16(float lo, float hi) { v2bf r = __builtin_convertvector((v2f){lo, hi}, v2bf); return __builtin_bit_cast(unsigned, r); }
__device__ __forceinline__ float rbf(float f) { return bf_lo(pk_bf16(f, f)); }
__device__ __forceinline__ float expd(float d) { return __builtin_amdgcn_exp2f(d * LOG2E); }
__device__ __forceinline__ v4f mfma(v4u a, v4u b, v4f c) { return __builtin_amdgcn_mfma_f32_16x16x32_bf16(__builtin_bit_cast(v8bf, a), __builtin_bit_cast(v8bf, b), c, 0, 0, 0); }
__device__ __forceinline__ v2u tr_read(unsigned addr) { return __builtin_bit_cast(v2u, __builtin_amdgcn_ds_read_tr16_b64_v4i16((LAS v4s*)addr)); }
__device__ __forceinline__ v4u tr2(unsigned a0, unsigned a1) { v2u x = tr_read(a0), y = tr_read(a1); return (v4u){x.x, x.y, y.x, y.y}; }
__device__ __forceinline__ v4u lds128(unsigned addr) { return *(LAS v4u*)addr; }
__device__ __forceinline__ v4u lds64x2(unsigned addr) { v2u x = *(LAS v2u*)addr, y = *(LAS v2u*)(addr + 32); return (v4u){x.x, x.y, y.x, y.y}; }
__device__ __forceinline__ float vmax(float a, float b) { return __builtin_elementwise_maximum(a, b); }
__device__ __forceinline__ float vmax3(float a, float b, float c) { return vmax(vmax(a, b), c); }
template <int CTRL> __device__ __forceinline__ float dpp(float x) { return asf(__builtin_amdgcn_mov_dpp(asu(x), CTRL, 0xf, 0xf, true)); }
__device__ __forceinline__ float row_sum(float x) { x += dpp<0x128>(x); x += dpp<0x124>(x); x += dpp<0x122>(x); x += dpp<0x121>(x); return x; }
__device__ __forceinline__ float sw32_add(float a, float b) { auto s = __builtin_amdgcn_permlane32_swap(asu(a), asu(b), false, false); return asf(s[0]) + asf(s[1]); }
__device__ __forceinline__ float sw16_add(float a, float b) { auto s = __builtin_amdgcn_permlane16_swap(asu(a), asu(b), false, false); return asf(s[0]) + asf(s[1]); }
__device__ __forceinline__ float x16_max(float x) { auto s = __builtin_amdgcn_permlane16_swap(asu(x), asu(x), false, false); return vmax(asf(s[0]), asf(s[1])); }
__device__ __forceinline__ float x32_max(float x) { auto s = __builtin_amdgcn_permlane32_swap(asu(x), asu(x), false, false); return vmax(asf(s[0]), asf(s[1])); }
__device__ __forceinline__ float xrow_sum(float x) { x = sw16_add(x, x); return sw32_add(x, x); }
__device__ __forceinline__ float xrow_max(float x) { return x32_max(x16_max(x)); }
__device__ __forceinline__ unsigned lds_ld32(unsigned addr) { return *(volatile LAS unsigned*)addr; }
__device__ __forceinline__ unsigned uni(unsigned v) { return __builtin_amdgcn_readfirstlane(v); }
__device__ __forceinline__ void lds_wait_ge(unsigned addr, unsigned v) {
  while (uni(lds_ld32(addr)) < v) { }
  FENCE();
}
__device__ __forceinline__ void lds_inc(unsigned addr_v) {
  asm volatile("s_mov_b64 exec, 1\n\tds_add_u32 %0, %1\n\ts_mov_b64 exec, -1" :: "v"(addr_v), "v"(1u) : "memory");
}
__device__ __forceinline__ void lds_st32(unsigned addr_v, unsigned val_v) { asm volatile("ds_write_b32 %0, %1" :: "v"(addr_v), "v"(val_v) : "memory"); }
__device__ __forceinline__ v4u ld_sc1(__amdgpu_buffer_rsrc_t rs, unsigned off) { return __builtin_amdgcn_raw_buffer_load_b128(rs, off, 0, 16); }
__device__ __forceinline__ unsigned tag_bad(v4u v, unsigned tag) { return __builtin_amdgcn_bitop3_b32(v.y, v.w, tag, 0x7e); }
__device__ __forceinline__ bool any_nz(unsigned bad) { return __builtin_amdgcn_ballot_w64(bad != 0) != 0; }
__device__ __forceinline__ unsigned now() { return (unsigned)__builtin_amdgcn_s_memrealtime(); }
__device__ __forceinline__ void wait_time(unsigned t0, unsigned ticks) { while (now() - t0 < ticks) { } }

__global__ void __launch_bounds__(384, 2) fused(char* ws_arg, const char* x, const char* wq, const char* kc, const char* vc,
                                                const char* w1, const char* wo, const char* w2, char* out) {
  __shared__ __attribute__((aligned(16))) char lds[L_END];
  asm volatile("" :: "v"(lds) : "memory");
  v4u xv; unsigned ep, cw0;
  u64 tk;
  char* ws;
  asm volatile(
      "s_load_dwordx16 s[36:51], %[ka], 0\n\t"
      "v_readfirstlane_b32 s52, %[tid]\n\t"
      "s_and_b32 s53, %[bid], 7\n\t"
      "s_lshr_b32 s54, %[bid], 3\n\t"
      "s_lshl_b32 s69, s53, 24\n\t"
      "s_lshr_b32 s52, s52, 6\n\t"
      "s_cbranch_scc0 3f\n\t"
      "v_and_b32 v100, 63, %[tid]\n\t"
      "s_cmp_lg_u32 s52, 5\n\t"
      "s_cbranch_scc1 4f\n\t"
      "s_getpc_b64 s[56:57]\n\t"
      "s_add_u32 s55, s54, %[pfh]+32\n\t"
      "s_and_b32 s56, s56, 0xffffff00\n\t"
      "s_cmp_le_u32 s55, %[cl]\n\t"
      "s_cselect_b32 s55, 32, 0\n\t"
      "v_and_b32 v101, s55, v100\n\t"
      "v_cmp_gt_u32 vcc, %[pfh], v100\n\t"
      "v_add3_u32 v101, v101, s54, %[pfh]\n\t"
      "s_nop 0\n\t"
      "v_cndmask_b32 v101, v101, v100, vcc\n\t"
      "v_lshlrev_b32 v101, 7, v101\n\t"
      "global_load_dword %[cw], v101, s[56:57]\n\t"
      "v_min_u32 v101, %[nz], v100\n\t"
      "v_lshlrev_b32 v101, 2, v101\n\t"
      "v_add_u32 v101, %[zb], v101\n\t"
      "v_mov_b32 v102, 0\n\t"
      "ds_write_b32 v101, v102\n"
      "4:\n\t"
      "v_lshlrev_b32 v101, 4, v100\n\t"
      "s_and_b32 s55, s54, 15\n\t"
      "s_lshl3_add_u32 s55, s55, s52\n\t"
      "s_add_u32 s64, s69, 0xeff000\n\t"
      "s_sub_u32 s58, s55, 3\n\t"
      "s_cmp_le_u32 s58, 1\n\t"
      "s_cselect_b32 s64, s64, 0\n\t"
      "s_waitcnt lgkmcnt(0)\n\t"
      "global_load_dwordx4 %[xv], v101, s[38:39]\n\t"
      "s_memrealtime vcc\n\t"
      "s_cselect_b64 s[56:57], s[42:43], s[36:37]\n\t"
      "s_cmp_eq_u32 s55, 4\n\t"
      "s_cselect_b64 s[56:57], s[44:45], s[56:57]\n\t"
      "s_cbranch_scc0 9f\n\t"
      "s_waitcnt lgkmcnt(0)\n\t"
      "s_mov_b64 %[tk], vcc\n\t"
      "s_mov_b64 %[wo_], s[36:37]\n\t"
      "s_barrier\n\t"
      "s_sleep 4\n\t"
      "s_load_dword %[ep], s[56:57], s64\n\t"
      "s_branch 1f\n"
      "9:\n\t"
      "s_lshl3_add_u32 s55, s54, s52\n\t"
      "s_waitcnt lgkmcnt(0)\n\t"
      "s_load_dword %[ep], s[56:57], s64\n\t"
      "s_mov_b64 %[tk], vcc\n\t"
      "s_mov_b64 %[wo_], s[36:37]\n\t"
      "s_cmp_lg_u32 s55, 5\n\t"
      "s_cbranch_scc1 5f\n\t"
      "v_and_b32 v100, 15, v100\n\t"
      "v_lshlrev_b32 v100, 3, v100\n\t"
      "v_lshl_add_u32 v100, s53, 7, v100\n\t"
      "v_mov_b32 v102, vcc_lo\n\t"
      "v_mov_b32 v103, vcc_hi\n\t"
      "global_store_dwordx2 v100, v[102:103], s[36:37] offset:%[wt] sc1\n"
      "5:\n\t"
      "s_barrier\n\t"
      "s_branch 1f\n"
      "3:\n\t"
      "v_lshrrev_b32 v100, 3, %[tid]\n\t"
      "v_and_b32 v101, 6, v100\n\t"
      "v_xor_b32 v101, v101, %[tid]\n\t"
      "v_and_b32 v101, 7, v101\n\t"
      "v_lshlrev_b32 v101, 4, v101\n\t"
      "s_and_b32 s55, s54, 3\n\t"
      "s_lshl_b32 s55, s55, 10\n\t"
      "s_xor_b32 s64, s53, 1\n\t"
      "s_or_b32 s55, s55, s64\n\t"
      "s_lshr_b32 s64, s54, 2\n\t"
      "s_lshl3_add_u32 s55, s55, s64\n\t"
      "s_lshl_b32 s55, s55, 7\n\t"
      "v_lshl_or_b32 v104, v100, 13, v101\n\t"
      "v_add_u32 v104, s55, v104\n\t"
      "v_add_u32 v105, 64512, v104\n\t"
      "v_add_u32 v106, 2*64512, v104\n\t"
      "v_add_u32 v107, 3*64512, v104\n\t"
      "s_mov_b32 m0, 0\n\t"
      "s_mov_b32 s59, 0x40000\n\t"
      "s_movk_i32 s58, 3\n\t"
      "s_waitcnt lgkmcnt(0)\n\t"
      "s_memrealtime s[62:63]\n"
      "2:\n\t"
      "global_load_lds_dwordx4 v104, s[40:41] nt\n\tv_add_u32 v104, s59, v104\n\t"
      "global_load_lds_dwordx4 v105, s[40:41] offset:1024 nt\n\tv_add_u32 v105, s59, v105\n\t"
      "global_load_lds_dwordx4 v106, s[40:41] offset:2048 nt\n\tv_add_u32 v106, s59, v106\n\t"
      "global_load_lds_dwordx4 v107, s[40:41] offset:3072 nt\n\tv_add_u32 v107, s59, v107\n\t"
      "s_add_u32 m0, m0, 0x1000\n\ts_sub_u32 s58, s58, 1\n\ts_cbranch_scc0 2b\n\t"
      "s_setprio 3\n\t"
      "s_barrier\n\t"
      "v_lshrrev_b32 v102, 4, %[tid]\n\t"
      "s_lshl_b32 s55, s54, 10\n\t"
      "s_xor_b32 s55, s55, 0xfe7000\n\t"
      "s_or_b32 s55, s55, s69\n\t"
      "v_lshl_or_b32 v111, v102, 8, s55\n\t"
      "v_xor_b32 v103, v102, %[tid]\n\t"
      "v_and_b32 v103, 15, v103\n\t"
      "v_lshl_or_b32 v104, v103, 4, v111\n\t"
      "v_xor_b32 v105, 0x8440, v104\n\tv_xor_b32 v106, 0x10880, v104\n\tv_xor_b32 v107, 0x18cc0, v104\n\t"
      "v_mov_b32 v108, 0\n\tv_mov_b32 v109, %[fl]\n\t"
      "s_movk_i32 s60, 16\n\ts_movk_i32 s61, %[s0]\n\ts_mov_b32 s68, 0x21000\n\t"
      "s_mov_b64 s[56:57], s[42:43]\n\ts_movk_i32 s58, 7\n\t"
      "s_call_b64 s[66:67], 6f\n\t"
      "v_lshlrev_b32 v103, 1, v102\n\t"
      "v_xor_b32 v103, v103, %[tid]\n\t"
      "v_and_b32 v103, 15, v103\n\t"
      "v_lshlrev_b32 v103, 4, v103\n\t"
      "v_or_b32 v104, v103, v111\n\t"
      "v_xor_b32 v105, 0x8480, v104\n\tv_xor_b32 v106, 0x10800, v104\n\tv_xor_b32 v107, 0x18c80, v104\n\t"
      "s_mov_b64 s[56:57], s[44:45]\n\ts_movk_i32 s58, 7\n\t"
      "s_call_b64 s[66:67], 6f\n\t"
      "s_sub_u32 s55, s54, 28\n\t"
      "s_cselect_b32 s55, s54, s55\n\t"
      "s_cselect_b32 s64, -1, 0\n\t"
      "s_mul_i32 s65, s53, 1792\n\t"
      "s_add_u32 s55, s55, s65\n\t"
      "s_lshl_b32 s55, s55, 7\n\t"
      "v_mul_u32_u24 v104, 3584, v100\n\t"
      "v_or_b32 v104, v104, v101\n\t"
      "v_and_b32 v104, s64, v104\n\t"
      "v_add_u32 v104, s55, v104\n\t"
      "v_add_u32 v105, 28672, v104\n\tv_add_u32 v106, 2*28672, v104\n\tv_add_u32 v107, 3*28672, v104\n\t"
      "v_mov_b32 v108, 4*28672\n\t"
      "s_mov_b32 s68, 0\n\ts_mov_b64 s[56:57], s[46:47]\n\ts_movk_i32 s58, 1\n\t"
      "s_call_b64 s[66:67], 6f\n\t"
      "s_add_u32 s55, s53, 2\n\t"
      "s_and_b32 s55, s55, 6\n\t"
      "s_and_b32 s64, s54, 15\n\t"
      "s_lshl3_add_u32 s55, s55, s64\n\t"
      "s_and_b32 s64, s53, 1\n\t"
      "s_lshr_b32 s65, s54, 4\n\t"
      "s_lshl1_add_u32 s64, s64, s65\n\t"
      "s_lshl_b32 s64, s64, 8\n\t"
      "v_lshl_or_b32 v111, v102, 10, v103\n\t"
      "v_or_b32 v111, s64, v111\n\t"
      "v_lshl_or_b32 v104, s55, 16, v111\n\t"
      "v_xor_b32 v105, 0x1080, v104\n\tv_xor_b32 v106, 0x2000, v104\n\tv_xor_b32 v107, 0x3080, v104\n\t"
      "v_mov_b32 v108, 0x4000\n\t"
      "s_mov_b64 s[56:57], s[48:49]\n\ts_movk_i32 s58, 3\n\t"
      "s_call_b64 s[66:67], 6f\n\t"
      "s_mul_i32 s55, s55, 28672\n\t"
      "s_add_u32 s56, s50, s55\n\ts_addc_u32 s57, s51, 0\n\t"
      "v_mov_b32 v104, v111\n\t"
      "v_xor_b32 v105, 0x1080, v111\n\tv_xor_b32 v106, 0x2000, v111\n\tv_xor_b32 v107, 0x3080, v111\n\t"
      "s_movk_i32 s58, 1\n"
      "6:\n\t"
      "s_add_u32 s59, s58, -1\n\ts_xor_b32 s59, s59, s58\n\ts_mul_i32 s59, s59, s68\n"
      "7:\n\t"
      "s_memrealtime vcc\n\t"
      "ds_read_b32 v112, v109 offset:24\n\t"
      "s_getreg_b32 s64, hwreg(HW_REG_IB_STS)\n\t"
      "s_and_b32 s65, s64, 15\n\ts_bfe_u32 s64, s64, 0x20016\n\ts_lshl4_add_u32 s65, s64, s65\n\t"
      "s_sub_u32 s64, s60, s65\n\tv_mov_b32 v110, s64\n\tds_write_b32 v109, v110\n\t"
      "s_cmp_le_u32 s65, %[hi]\n\ts_cselect_b32 s64, s61, -1\n\t"
      "s_cmp_le_u32 s65, %[lo]\n\ts_cselect_b32 s64, 0, s64\n\t"
      "s_waitcnt lgkmcnt(0)\n\t"
      "v_readfirstlane_b32 s65, v112\n\ts_cmp_lg_u32 s65, 0\n\ts_cselect_b32 s62, s65, s62\n\t"
      "s_sub_u32 s65, vcc_lo, s62\n\ts_lshl_b32 s65, s65, 2\n\t"
      "s_cmp_lt_u32 s65, s64\n\ts_cbranch_scc1 7b\n\t"
      "global_load_lds_dwordx4 v104, s[56:57] nt\n\ts_add_u32 m0, m0, 0x400\n\tv_xad_u32 v104, v104, s59, v108\n\t"
      "global_load_lds_dwordx4 v105, s[56:57] nt\n\ts_add_u32 m0, m0, 0x400\n\tv_xad_u32 v105, v105, s59, v108\n\t"
      "global_load_lds_dwordx4 v106, s[56:57] nt\n\ts_add_u32 m0, m0, 0x400\n\tv_xad_u32 v106, v106, s59, v108\n\t"
      "s_cmp_eq_u32 s60, 108\n\ts_cbranch_scc1 8f\n\t"
      "global_load_lds_dwordx4 v107, s[56:57] nt\n\ts_add_u32 m0, m0, 0x400\n\tv_xad_u32 v107, v107, s59, v108\n\t"
      "s_add_u32 s60, s60, 4\n\ts_add_u32 s61, s61, %[g]\n\t"
      "s_sub_u32 s58, s58, 1\n\ts_cbranch_scc0 6b\n\t"
      "s_setpc_b64 s[66:67]\n"
      "8:\n\t"
      "s_getreg_b32 s64, hwreg(HW_REG_IB_STS)\n\t"
      "s_and_b32 s65, s64, 15\n\ts_bfe_u32 s64, s64, 0x20016\n\ts_lshl4_add_u32 s65, s64, s65\n\t"
      "s_sub_u32 s64, 111, s65\n\tv_mov_b32 v110, s64\n\tds_write_b32 v109, v110\n\t"
      "s_waitcnt lgkmcnt(0)\n\t"
      "s_cmp_gt_u32 s65, 23\n\ts_cbranch_scc1 8b\n\t"
      "v_mov_b32 v110, 111\n\ts_waitcnt vmcnt(0)\n\tds_write_b32 v109, v110\n\t"
      "s_endpgm\n"
      "1:"
      : [xv] "=&v"(xv), [ep] "=&s"(ep), [tk] "=&s"(tk), [cw] "=&v"(cw0), [wo_] "=&s"(ws)
      : [ka] "s"(__builtin_amdgcn_kernarg_segment_ptr()), [tid] "v"(threadIdx.x), [bid] "s"(blockIdx.x),
        [cl] "i"(CODE_LINES - 1), [nz] "i"(NZW - 1), [zb] "i"(L_A + 184), [wt] "i"(W_TREF), [pfh] "i"(PFH), [fl] "i"(F_LOAD),
        [s0] "i"(4 * (SCH_B) + 4 * SCH_G), [lo] "i"(LOWW), [hi] "i"(HIGHW), [g] "i"(SCH_G)
      : "memory", "scc", "vcc", "s36", "s37", "s38", "s39", "s40", "s41", "s42", "s43", "s44", "s45", "s46", "s47", "s48", "s49", "s50", "s51", "s52", "s53",
        "s54", "s55", "s56", "s57", "s58", "s59", "s60", "s61", "s62", "s63", "s64", "s65", "s66", "s67", "s68", "s69",
        "v100", "v101", "v102", "v103", "v104", "v105", "v106", "v107", "v108", "v109", "v110", "v111", "v112");
  __builtin_amdgcn_sched_barrier(0);
  const int wave = __builtin_amdgcn_readfirstlane(threadIdx.x >> 6), lane = threadIdx.x & 63;
  const int b = blockIdx.x, g = b & 7, s = b >> 3;
  const int g4 = lane >> 4, r16 = lane & 15;
  const int gq = g ^ 1;
  const int rp = s & 7, gc = (((g & 6) + 2) & 6) + ((s >> 3) & 1), cg = 2 * (g & 1) + (s >> 4), pi = gc * 8 + rp;
  const unsigned l16 = lane * 16;
  ws = (char*)(((u64)uni((unsigned)((u64)ws >> 32)) << 32) | uni((unsigned)(u64)ws));
  const __amdgpu_buffer_rsrc_t rs = __builtin_amdgcn_make_buffer_rsrc(ws, 0, W_END, 0x00020000);
  const v4f z = {0, 0, 0, 0};
  if (__builtin_expect(wave != 5, 1)) {
    const int w = wave - 1;
    const int q4 = r16 >> 2, p4 = lane & 3;
    const unsigned tr128 = (4 * g4 + q4) * 128 + (((p4 >> 1) ^ (((2 * g4 + (q4 >> 1)) & 3) << 1)) * 16) + (p4 & 1) * 8;
    unsigned wqa = L_WQ + (tr128 ^ (64 * (w & 1))), fL = F_LOAD;
    unsigned sq = lane < 16 ? W_QG + gq * 8192 + (s >> 3) * 2048 + (s & 3) * 512 + (((s >> 2) & 1) * 2 + w) * 128 + lane * 8 : OOB;
    unsigned xw = L_XN + w * 1024 + l16, xq = L_XN + w * 1024 + (128 * (s & 3) + 4 * g4) * 2, x1 = L_XN + w * 1024 + (64 * g + 4 * g4) * 2;
    asm volatile("" : "+v"(wqa), "+v"(sq), "+v"(xw), "+v"(xq), "+v"(x1), "+v"(fL));
    __builtin_amdgcn_sched_barrier(0);
    asm volatile("s_waitcnt vmcnt(0) lgkmcnt(0)" ::: "memory");
    __builtin_amdgcn_sched_barrier(0);
    asm volatile("" : "+v"(xv), "+s"(ep), "+s"(tk) :: "memory");
    const unsigned tref = (unsigned)tk;
    unsigned tag = ep * 1664525u + 1013904223u;
    asm volatile("" : "+s"(tag));
    if (w == 0) { lds_st32(F_T, tag); lds_st32(F_D, 1); }
    v4u aq[4], a1[2];
    {
      float f[8] = {bf_lo(xv.x), bf_hi(xv.x), bf_lo(xv.y), bf_hi(xv.y), bf_lo(xv.z), bf_hi(xv.z), bf_lo(xv.w), bf_hi(xv.w)};
      float sm = ((f[0] + f[1]) + (f[2] + f[3])) + ((f[4] + f[5]) + (f[6] + f[7]));
      const float mean = xrow_sum(row_sum(sm)) * (1.f / 512.f);
      float vs = 0.f;
#pragma unroll
      for (int i = 0; i < 8; ++i) { float d = f[i] - mean; vs = fmaf(d, d, vs); }
      const float var = xrow_sum(row_sum(vs)) * (1.f / 512.f);
      const float mb_ = rbf(mean), rden = __builtin_amdgcn_rcpf(rbf(__builtin_amdgcn_sqrtf(rbf(rbf(var) + 1e-5f))));
      v4u xn;
#pragma unroll
      for (int i = 0; i < 4; ++i) { const unsigned t = pk_bf16(f[2 * i] - mb_, f[2 * i + 1] - mb_); xn[i] = pk_bf16(bf_lo(t) * rden, bf_hi(t) * rden); }
      *(LAS v4u*)xw = xn;
#pragma unroll
      for (int ks = 0; ks < 4; ++ks) aq[ks] = lds64x2(xq + 64 * ks);
#pragma unroll
      for (int ks = 0; ks < 2; ++ks) a1[ks] = lds64x2(x1 + 64 * ks);
    }
    if (w < 2) {
      lds_wait_ge(fL, M_WQ);
      v4f c0 = z, c1 = z;
#pragma unroll
      for (int ks = 0; ks < 4; ++ks) {
        c0 = mfma(aq[ks], tr2(wqa + ks * 4096, wqa + ks * 4096 + 2048), c0);
        c1 = mfma(aq[ks], tr2((wqa ^ 32) + ks * 4096, (wqa ^ 32) + ks * 4096 + 2048), c1);
      }
      const unsigned ua = asu(c0.x), ub = asu(c1.x) + 0x80u;
      __builtin_amdgcn_raw_buffer_store_b64((v2u){__builtin_amdgcn_perm(ua, ub, 0x07060501u), __builtin_amdgcn_perm(ub, tag, 0x07060100u)}, rs, sq, 0, 16);
    } else {
      lds_wait_ge(F_D, 1);
      tag = uni(lds_ld32(F_T));
    }
    __builtin_amdgcn_sched_barrier(0);
    const unsigned tv0 = (4 * g4 + q4) * 256 + (((4 * w + (p4 >> 1)) ^ (((4 * g4 + q4) & 7) << 1)) * 16) + (p4 & 1) * 8;
    const unsigned tv1 = tv0 ^ 32;
    unsigned sh = (lane < 16 && s < 28) ? W_HG + (g * 1792 + s * 64 + w * 16 + lane) * 8 : OOB;
    unsigned s3 = lane < 16 ? W_P3 + (cg * 64 + pi) * 1024 + (w * 16 + lane) * 16 : OOB;
    unsigned so = W_OG + (g * 32 + s) * 2048 + (g4 * 4 + w) * 128 + r16 * 8;
    unsigned ob = W_OG + (gc * 32 + (lane >> 2)) * 2048 + (rp * 2 + (w >> 1)) * 128 + (w & 1) * 64 + p4 * 16;
    unsigned qb = W_QG + g * 8192 + w * 2048 + l16;
    unsigned mb = W_ML + (gc * 32 + (lane >> 2)) * 128 + (rp >> 1) * 16;
    asm volatile("" : "+v"(sh), "+v"(s3), "+v"(so), "+v"(ob), "+v"(qb), "+v"(mb));
    unsigned ka[4], qa = L_Q + p4 * 272 + g4 * 16, qw = L_Q + w * 272 + (32 * ((lane >> 3) & 3) + 16 * (lane >> 5) + 2 * (lane & 7)) * 2;
    unsigned pw = L_P + ((w * 4 + g4) * 4 + p4) * 16, mw = L_M + (p4 * 4 + w) * 4, pr = L_P + (g4 * 4 + q4) * 16, mr = L_M + g4 * 16;
    unsigned va0 = L_V + tv0, va1 = L_V + tv1, wa0 = L_WO + tv0, wa1 = L_WO + tv1, w1a = L_W1 + (tr128 ^ (32 * w));
    unsigned wa0h = wa0 + 16384 + (g4 == 3 ? 0u : 4096u), wa1h = wa1 + 16384 + (g4 == 3 ? 0u : 4096u);
    unsigned aw = L_A + (32 * (w >> 1) + 8 * (2 * (w & 1) + (p4 >> 1)) + 4 * (g4 & 1) + 2 * (p4 & 1) + (g4 >> 1)) * 2, ar = L_A + g4 * 16;
#pragma unroll
    for (int ks = 0; ks < 4; ++ks) ka[ks] = L_K + (32 * w + r16) * 256 + (((4 * ks + g4) ^ r16) * 16);
    unsigned fQ = F_Q, fP = F_P, fA = F_A;
    asm volatile("" : "+v"(ka[0]), "+v"(ka[1]), "+v"(ka[2]), "+v"(ka[3]), "+v"(qa), "+v"(qw), "+v"(pw), "+v"(mw), "+v"(pr), "+v"(mr));
    asm volatile("" : "+v"(va0), "+v"(va1), "+v"(wa0), "+v"(wa1), "+v"(w1a), "+v"(wa0h), "+v"(wa1h), "+v"(aw), "+v"(ar), "+v"(fQ), "+v"(fP), "+v"(fA));
    NOUNROLL for (;;) {
      const unsigned fr = uni(lds_ld32(F_R1)), fl = lds_ld32(fL), t = now();
      if ((fr ? (int)(t - fr) >= 0 : t - tref >= T_FB) || uni(fl) >= M_H1) break;
    }
    v4u kf[2][4];
    {
      FENCE();
      v4u v0 = ld_sc1(rs, qb), v1 = ld_sc1(rs, qb + 1024);
      lds_wait_ge(fL, M_K);
#pragma unroll
      for (int ks = 0; ks < 4; ++ks)
#pragma unroll
        for (int T = 0; T < 2; ++T) kf[T][ks] = lds128(ka[ks] + T * 4096);
      FENCE();
      if (UNLIKELY(any_nz((tag_bad(v0, tag) | tag_bad(v1, tag)) << 16))) {
        unsigned np = 0;
        NOUNROLL do {
          FENCE();
          v0 = ld_sc1(rs, qb); v1 = ld_sc1(rs, qb + 1024);
        } while (any_nz((tag_bad(v0, tag) | tag_bad(v1, tag)) << 16) && ++np < SPIN_CAP);
      }
      const float a0 = asf(v0.x) + asf(v1.x), a2 = asf(v0.z) + asf(v1.z);
      const float a1_ = asf(__builtin_amdgcn_perm(v0.y, v0.x, 0x0706000cu)) + asf(__builtin_amdgcn_perm(v1.y, v1.x, 0x0706000cu));
      const float a3 = asf(__builtin_amdgcn_perm(v0.w, v0.z, 0x0706000cu)) + asf(__builtin_amdgcn_perm(v1.w, v1.z, 0x0706000cu));
      *(volatile LAS unsigned*)qw = pk_bf16(sw32_add(a0, a1_), sw32_add(a2, a3));
      lds_inc(fQ);
    }
    v4u vb0[4], vb1[4];
    unsigned fv;
    {
      v4u qf[4];
      NOUNROLL for (;;) {
        const unsigned f1 = lds_ld32(fQ);
        FENCE();
#pragma unroll
        for (int ks = 0; ks < 4; ++ks) qf[ks] = lds128(qa + 64 * ks);
        FENCE();
        if (uni(f1) >= 4) break;
      }
      v4f s0 = z, s1 = z;
#pragma unroll
      for (int ks = 0; ks < 4; ++ks) { s0 = mfma(kf[0][ks], qf[ks], s0); s1 = mfma(kf[1][ks], qf[ks], s1); }
      float sc[8] = {s0.x, s0.y, s0.z, s0.w, s1.x, s1.y, s1.z, s1.w};
#pragma unroll
      for (int i = 0; i < 4; ++i) {
        unsigned t = pk_bf16(sc[2 * i], sc[2 * i + 1]);
        t = pk_bf16(bf_lo(t) * 0.08838834764831845f, bf_hi(t) * 0.08838834764831845f);
        sc[2 * i] = bf_lo(t); sc[2 * i + 1] = bf_hi(t);
      }
      float mx = vmax3(vmax3(vmax3(sc[0], sc[1], sc[2]), sc[3], sc[4]), vmax3(sc[5], sc[6], sc[7]), -3.0e38f);
      mx = xrow_max(mx);
      *(volatile LAS float*)mw = mx;
      fv = lds_ld32(fL);
      FENCE();
#pragma unroll
      for (int ks = 0; ks < 4; ++ks) { vb0[ks] = tr2(va0 + ks * 8192, va0 + ks * 8192 + 4096); vb1[ks] = tr2(va1 + ks * 8192, va1 + ks * 8192 + 4096); }
      FENCE();
      v4u pf;
#pragma unroll
      for (int i = 0; i < 4; ++i) {
        const v2f d = ((v2f){sc[2 * i], sc[2 * i + 1]} - (v2f){mx, mx}) * (v2f){LOG2E, LOG2E};
        pf[i] = pk_bf16(__builtin_amdgcn_exp2f(d.x), __builtin_amdgcn_exp2f(d.y));
      }
      *(volatile LAS v4u*)pw = pf;
      lds_inc(fP);
    }
    {
      v4u pa[4], m4;
      NOUNROLL for (;;) {
        const unsigned f = lds_ld32(fP);
        FENCE();
        m4 = lds128(mr);
#pragma unroll
        for (int ks = 0; ks < 4; ++ks) pa[ks] = lds128(pr + ks * 256);
        FENCE();
        if (uni(f) >= 4) break;
      }
      const float M = vmax(vmax3(asf(m4.x), asf(m4.y), asf(m4.z)), asf(m4.w));
      float al[4] = {expd(asf(m4.x) - M), expd(asf(m4.y) - M), expd(asf(m4.z) - M), expd(asf(m4.w) - M)};
      const unsigned fvu = uni(fv);
      if (fvu < M_V2) {
        lds_wait_ge(fL, M_V2);
#pragma unroll
        for (int ks = 0; ks < 3; ++ks) { vb0[ks] = tr2(va0 + ks * 8192, va0 + ks * 8192 + 4096); vb1[ks] = tr2(va1 + ks * 8192, va1 + ks * 8192 + 4096); }
      }
      float o0 = 0.f, o1 = 0.f;
#pragma unroll
      for (int ks = 0; ks < 3; ++ks) { v4f d0 = mfma(pa[ks], vb0[ks], z), d1 = mfma(pa[ks], vb1[ks], z); o0 = fmaf(al[ks], d0.x, o0); o1 = fmaf(al[ks], d1.x, o1); }
      asm volatile("" : "+v"(o0), "+v"(o1), "+v"(al[3]));
      if (fvu < M_V3) {
        NOUNROLL for (;;) {
          const unsigned f = lds_ld32(fL);
          FENCE();
          vb0[3] = tr2(va0 + 3 * 8192, va0 + 3 * 8192 + 4096); vb1[3] = tr2(va1 + 3 * 8192, va1 + 3 * 8192 + 4096);
          FENCE();
          if (uni(f) >= M_V3) break;
        }
      }
      v4f d0 = mfma(pa[3], vb0[3], z), d1 = mfma(pa[3], vb1[3], z);
      o0 = fmaf(al[3], d0.x, o0); o1 = fmaf(al[3], d1.x, o1);
      __builtin_amdgcn_raw_buffer_store_b64((v2u){pk_bf16(o0, o1), tag}, rs, so, 0, 16);
    }
    {
      v4u bh[2];
      NOUNROLL for (;;) {
        const unsigned f = lds_ld32(fL);
        FENCE();
#pragma unroll
        for (int ks = 0; ks < 2; ++ks) bh[ks] = tr2(w1a + ks * 4096, w1a + ks * 4096 + 2048);
        FENCE();
        if (uni(f) >= M_W1) break;
      }
      v4f acc = mfma(a1[1], bh[1], mfma(a1[0], bh[0], z));
      __builtin_amdgcn_raw_buffer_store_b64((v2u){asu(acc.x), tag}, rs, sh, 0, 16);
      if (w == 0) lds_st32(F_D, 2);
    }
    {
      unsigned gref, cnt, tmp;
      do { gref = uni(lds_ld32(F_R)); } while (gref == 0);
      asm volatile(
          ".macro RB\n\t"
          "ds_read_b64_tr_b16 v[60:61], %[b0]\n\tds_read_b64_tr_b16 v[62:63], %[b0] offset:4096\n\t"
          "ds_read_b64_tr_b16 v[64:65], %[b1]\n\tds_read_b64_tr_b16 v[66:67], %[b1] offset:4096\n\t"
          "ds_read_b64_tr_b16 v[68:69], %[b0] offset:8192\n\tds_read_b64_tr_b16 v[70:71], %[b0] offset:12288\n\t"
          "ds_read_b64_tr_b16 v[72:73], %[b1] offset:8192\n\tds_read_b64_tr_b16 v[74:75], %[b1] offset:12288\n\t"
          "ds_read_b64_tr_b16 v[76:77], %[b0] offset:16384\n\tds_read_b64_tr_b16 v[78:79], %[b0h]\n\t"
          "ds_read_b64_tr_b16 v[80:81], %[b1] offset:16384\n\tds_read_b64_tr_b16 v[82:83], %[b1h]\n\t"
          ".endm\n\t"
          "s_mov_b32 %[cnt], %[cap]\n\t"
          "v_mov_b32 v99, 1\n"
          "1:\n\t"
          "s_memrealtime vcc\n\ts_waitcnt lgkmcnt(0)\n\ts_sub_u32 vcc_lo, vcc_lo, %[ref]\n\ts_cmp_lt_u32 vcc_lo, %[t]\n\ts_cbranch_scc1 1b\n"
          "2:\n\t"
          "buffer_load_dwordx4 v[100:103], %[mb], %[rs], 0 offen sc1\n\t"
          "buffer_load_dwordx4 v[104:107], %[mb], %[rs], 0 offen offset:2048 sc1\n\t"
          "s_sleep %[dml]\n\t"
          "buffer_load_dwordx4 v[108:111], %[o0], %[rs], 0 offen sc1\n\t"
          "buffer_load_dwordx4 v[112:115], %[o1], %[rs], 0 offen sc1\n\t"
          "s_sub_u32 %[cnt], %[cnt], 1\n\t"
          "s_waitcnt vmcnt(2)\n\t"
          "v_max_f32 v96, v100, v104\n\t"
          "v_bitop3_b32 v101, v101, v103, %[tag] bitop3:0x7e\n\t"
          "v_bitop3_b32 v105, v105, v107, %[tag] bitop3:0x7e\n\t"
          "v_max_f32_dpp v96, v96, v96 row_ror:8 row_mask:0xf bank_mask:0xf\n\t"
          "v_or_b32 v101, v101, v105\n\t"
          "v_cmp_ne_u32 vcc, 0, v101\n\t"
          "v_max_f32_dpp v96, v96, v96 row_ror:4 row_mask:0xf bank_mask:0xf\n\t"
          "v_mov_b32 v97, v96\n\t"
          "s_cselect_b64 vcc, 0, vcc\n\t"
          "s_cbranch_vccnz 2b\n\t"
          "v_permlane16_swap_b32 v96, v97\n\t"
          "v_max_f32 v96, v96, v97\n\t"
          "v_mov_b32 v97, v96\n\t"
          "s_mov_b32 %[cnt], %[cap]\n\t"
          "s_nop 0\n\t"
          "v_permlane32_swap_b32 v96, v97\n\t"
          "v_max_f32 v96, v96, v97\n\t"
          "v_sub_f32 v100, v100, v96\n\t"
          "v_sub_f32 v104, v104, v96\n\t"
          "v_mul_f32 v100, 0x3fb8aa3b, v100\n\t"
          "v_mul_f32 v104, 0x3fb8aa3b, v104\n\t"
          "v_exp_f32 v100, v100\n\t"
          "v_exp_f32 v104, v104\n\t"
          "ds_read_b32 v98, %[fl]\n\t"
          "v_mul_f32 v96, v100, v102\n\t"
          "v_fmac_f32 v96, v104, v106\n\t"
          "ds_read_b64_tr_b16 v[60:61], %[b0]\n\tds_read_b64_tr_b16 v[62:63], %[b0] offset:4096\n\t"
          "v_add_f32_dpp v96, v96, v96 row_ror:8 row_mask:0xf bank_mask:0xf\n\t"
          "ds_read_b64_tr_b16 v[64:65], %[b1]\n\tds_read_b64_tr_b16 v[66:67], %[b1] offset:4096\n\t"
          "v_add_f32_dpp v96, v96, v96 row_ror:4 row_mask:0xf bank_mask:0xf\n\t"
          "v_mov_b32 v97, v96\n\t"
          "ds_read_b64_tr_b16 v[68:69], %[b0] offset:8192\n\tds_read_b64_tr_b16 v[70:71], %[b0] offset:12288\n\t"
          "v_permlane16_swap_b32 v96, v97\n\t"
          "v_add_f32 v96, v96, v97\n\t"
          "v_mov_b32 v97, v96\n\t"
          "ds_read_b64_tr_b16 v[72:73], %[b1] offset:8192\n\tds_read_b64_tr_b16 v[74:75], %[b1] offset:12288\n\t"
          "v_permlane32_swap_b32 v96, v97\n\t"
          "v_add_f32 v96, v96, v97\n\t"
          "v_rcp_f32 v96, v96\n\t"
          "ds_read_b64_tr_b16 v[76:77], %[b0] offset:16384\n\t"
          "v_mul_f32 v100, v100, v96\n\t"
          "v_mul_f32 v104, v104, v96\n"
          "3:\n\t"
          "s_waitcnt vmcnt(0)\n\t"
          "v_lshlrev_b32 v116, 16, v108\n\tv_and_b32 v117, 0xffff0000, v108\n\t"
          "v_lshlrev_b32 v118, 16, v110\n\tv_and_b32 v119, 0xffff0000, v110\n\t"
          "v_lshlrev_b32 v120, 16, v112\n\tv_and_b32 v121, 0xffff0000, v112\n\t"
          "v_lshlrev_b32 v122, 16, v114\n\tv_and_b32 v123, 0xffff0000, v114\n\t"
          "v_pk_mul_f32 v[116:117], v[116:117], v[100:101] op_sel_hi:[1,0]\n\t"
          "v_pk_mul_f32 v[118:119], v[118:119], v[100:101] op_sel_hi:[1,0]\n\t"
          "v_pk_fma_f32 v[116:117], v[120:121], v[104:105], v[116:117] op_sel_hi:[1,0,1]\n\t"
          "v_pk_fma_f32 v[118:119], v[122:123], v[104:105], v[118:119] op_sel_hi:[1,0,1]\n\t"
          "v_bitop3_b32 v109, v109, v111, %[tag] bitop3:0x7e\n\t"
          "v_bitop3_b32 v113, v113, v115, %[tag] bitop3:0x7e\n\t"
          "v_permlane32_swap_b32 v116, v118\n\t"
          "v_permlane32_swap_b32 v117, v119\n\t"
          "v_add_f32 v116, v116, v118\n\t"
          "v_add_f32 v117, v117, v119\n\t"
          "v_or_b32 v109, v109, v113\n\t"
          "v_cmp_ne_u32 vcc, 0, v109\n\t"
          "v_permlane16_swap_b32 v116, v117\n\t"
          "v_add_f32 v116, v116, v117\n\t"
          "s_cbranch_vccnz 8f\n"
          "4:\n\t"
          "ds_read_b64_tr_b16 v[78:79], %[b0h]\n\t"
          "v_add_f32_dpp v116, v116, v116 row_ror:8 row_mask:0xf bank_mask:0xf\n\t"
          "ds_read_b64_tr_b16 v[80:81], %[b1] offset:16384\n\tds_read_b64_tr_b16 v[82:83], %[b1h]\n\t"
          "v_add_f32_dpp v116, v116, v116 row_ror:4 row_mask:0xf bank_mask:0xf\n\t"
          "v_cvt_pk_bf16_f32 v116, v116, v116\n\t"
          "ds_write_b16 %[aw], v116\n\t"
          "ds_write_b8 %[fw], v99\n"
          "5:\n\t"
          "ds_read_b64 v[96:97], %[fa]\n\t"
          "ds_read_b128 v[84:87], %[ar]\n\t"
          "ds_read_b128 v[88:91], %[ar] offset:64\n\t"
          "ds_read_b128 v[92:95], %[ar] offset:128\n\t"
          "s_waitcnt lgkmcnt(3)\n\t"
          "v_readfirstlane_b32 %[tmp], v96\n\t"
          "s_cmp_lg_u32 %[tmp], 0x01010101\n\t"
          "s_cbranch_scc1 5b\n\t"
          "v_readfirstlane_b32 %[tmp], v98\n\t"
          "s_cmp_lt_u32 %[tmp], %[all]\n\t"
          "s_cbranch_scc1 9f\n"
          "6:\n\t"
          "v_readfirstlane_b32 %[tmp], v97\n\t"
          "s_waitcnt lgkmcnt(2)\n\t"
          "v_mfma_f32_16x16x32_bf16 v[100:103], v[84:87], v[60:63], 0\n\t"
          "v_mfma_f32_16x16x32_bf16 v[104:107], v[84:87], v[64:67], 0\n\t"
          "s_cmp_lg_u32 %[tmp], 0\n\t"
          "s_waitcnt lgkmcnt(1)\n\t"
          "v_mfma_f32_16x16x32_bf16 v[100:103], v[88:91], v[68:71], v[100:103]\n\t"
          "v_mfma_f32_16x16x32_bf16 v[104:107], v[88:91], v[72:75], v[104:107]\n\t"
          "s_cbranch_scc1 7f\n"
          "10:\n\t"
          "ds_read_b32 v97, %[fa] offset:4\n\t"
          "ds_read_b128 v[92:95], %[ar] offset:128\n\t"
          "s_waitcnt lgkmcnt(1)\n\t"
          "v_readfirstlane_b32 %[tmp], v97\n\t"
          "s_cmp_eq_u32 %[tmp], 0\n\t"
          "s_cbranch_scc1 10b\n"
          "7:\n\t"
          "s_waitcnt lgkmcnt(0)\n\t"
          "v_mfma_f32_16x16x32_bf16 v[100:103], v[92:95], v[76:79], v[100:103]\n\t"
          "v_mfma_f32_16x16x32_bf16 v[104:107], v[92:95], v[80:83], v[104:107]\n\t"
          "v_mov_b32 v109, %[tag]\n\t"
          "v_mov_b32 v111, %[tag]\n\t"
          "s_nop 4\n\t"
          "v_mov_b32 v108, v100\n\t"
          "v_mov_b32 v110, v104\n\t"
          "buffer_store_dwordx4 v[108:111], %[s3], %[rs], 0 offen sc1\n\t"
          "s_branch 11f\n"
          "8:\n\t"
          "s_sub_u32 %[cnt], %[cnt], 1\n\t"
          "s_cbranch_scc1 4b\n\t"
          "buffer_load_dwordx4 v[108:111], %[o0], %[rs], 0 offen sc1\n\t"
          "buffer_load_dwordx4 v[112:115], %[o1], %[rs], 0 offen sc1\n\t"
          "s_branch 3b\n"
          "9:\n\t"
          "ds_read_b32 v98, %[fl]\n\t"
          "s_waitcnt lgkmcnt(0)\n\t"
          "v_readfirstlane_b32 %[tmp], v98\n\t"
          "s_cmp_lt_u32 %[tmp], %[all]\n\t"
          "s_cbranch_scc1 9b\n\t"
          "RB\n\t"
          "s_waitcnt lgkmcnt(0)\n\t"
          "s_branch 6b\n"
          "11:\n\t"
          "s_nop 0\n\t"
          ".purgem RB"
          : [cnt] "=&s"(cnt), [tmp] "=&s"(tmp)
          : [cap] "i"(SPIN_CAP), [ref] "s"(gref), [t] "i"(T_H2), [dml] "i"(D_ML), [all] "i"(M_ALL), [rs] "s"(rs), [tag] "s"(tag),
            [mb] "v"(mb), [o0] "v"(ob), [o1] "v"(ob + 32768), [fl] "v"(fL), [b0] "v"(wa0), [b1] "v"(wa1), [b0h] "v"(wa0h), [b1h] "v"(wa1h),
            [aw] "v"(aw), [fw] "v"(fA + w), [fa] "v"(fA), [ar] "v"(ar), [s3] "v"(s3)
          : "memory", "scc", "vcc", "v60", "v61", "v62", "v63", "v64", "v65", "v66", "v67", "v68", "v69", "v70", "v71", "v72", "v73", "v74", "v75",
            "v76", "v77", "v78", "v79", "v80", "v81", "v82", "v83", "v84", "v85", "v86", "v87", "v88", "v89", "v90", "v91", "v92", "v93", "v94", "v95",
            "v96", "v97", "v98", "v99", "v100", "v101", "v102", "v103", "v104", "v105", "v106", "v107", "v108", "v109", "v110", "v111", "v112", "v113",
            "v114", "v115", "v116", "v117", "v118", "v119", "v120", "v121", "v122", "v123");
      if (w == 0) lds_st32(F_D, 3);
    }
    __builtin_amdgcn_endpgm();
  }
  const int cgc = 2 * (1 - (g & 1)) + (s >> 4), slot = (g >> 1) * 16 + (s & 15);
  const int c0 = 128 * cgc + 32 * (slot >> 4) + (slot & 15);
  asm volatile("s_waitcnt vmcnt(0) lgkmcnt(0)" ::: "memory");
  __builtin_amdgcn_sched_barrier(0);
  asm volatile("" : "+v"(xv), "+s"(ep), "+s"(tk), "+v"(cw0) :: "memory");
  const unsigned tref = (unsigned)tk;
  unsigned xa, xb;
  {
    const int dw = (c0 >> 1) & 3;
    const unsigned sel = dw == 0 ? xv.x : dw == 1 ? xv.y : dw == 2 ? xv.z : xv.w;
    xa = __builtin_amdgcn_readlane(sel, c0 >> 3); xb = __builtin_amdgcn_readlane(sel, (c0 >> 3) + 2);
    if (c0 & 1) { xa >>= 16; xb >>= 16; }
  }
  const int hr = r16 < 14 ? r16 : 13;
  unsigned pb = W_P3 + (cgc * 64 + lane) * 1024 + slot * 16;
  unsigned hb = W_HG + (g4 * 1792 + pi * 28 + hr * 2) * 8, hw = L_A + (64 + 8 * ((hr >> 1) & 3) + 4 * (hr >> 3) + 2 * (hr & 1) + (lane >> 5)) * 2;
  float xr = bf_lo(lane < 32 ? xa : xb);
  unsigned oo = (lane & 31) == 16 ? (c0 + (lane >> 5) * 16) * 2 : OOB;
  asm volatile("" : "+v"(pb), "+v"(hb), "+v"(hw), "+v"(xr), "+v"(oo));
  unsigned gref;
  {
    wait_time(tref, T_R);
    const v4u r = ld_sc1(rs, W_TREF + (lane & 7) * 128);
    const u64 d = (((u64)r.y << 32) | r.x) - tk + 256u;
    const unsigned key = (((d < 512u && r.x == r.z && r.y == r.w) ? (unsigned)d : 256u + T_INV) << 3) | (lane & 7);
    unsigned rank = 0;
#define RK(n) rank += (unsigned)__builtin_amdgcn_mov_dpp(key, 0x120 + n, 0xf, 0xf, true) < key;
    RK(1) RK(2) RK(3) RK(4) RK(5) RK(6) RK(7)
    const int kth = __builtin_amdgcn_readlane(key, __builtin_ctzll(__builtin_amdgcn_ballot_w64(rank == 3))) >> 3;
    const int top = __builtin_amdgcn_readlane(key, __builtin_ctzll(__builtin_amdgcn_ballot_w64(rank == 7))) >> 3;
    const int par = __builtin_amdgcn_readlane(key, gq) >> 3;
    lds_st32(F_R1, (tref - 256u + max(par + T_H1P, kth + T_H1M)) | 1u);
    gref = uni((tref - 256u + max(kth, top - T_LAG)) | 1u);
    lds_st32(F_R, gref);
  }
  asm volatile("" : "+s"(out));
  const __amdgpu_buffer_rsrc_t ro = __builtin_amdgcn_make_buffer_rsrc(out, 0, 1024, 0x00020000);
  lds_wait_ge(F_D, 1);
  const unsigned tag = uni(lds_ld32(F_T));
  {
    unsigned sm_ = r16 < 2 ? W_ML + (g * 32 + s) * 128 + r16 * 64 + g4 * 16 : OOB, pr = L_P + (g4 * 4 + (r16 >> 2)) * 16, mr = L_M + g4 * 16;
    asm volatile("" : "+v"(sm_), "+v"(pr), "+v"(mr));
    lds_wait_ge(F_P, 4);
    const v4u m4 = lds128(mr);
    const v4u ones = {0x3f803f80u, 0x3f803f80u, 0x3f803f80u, 0x3f803f80u};
    const float l0 = mfma(lds128(pr), ones, z).x, l1 = mfma(lds128(pr + 256), ones, z).x, l2 = mfma(lds128(pr + 512), ones, z).x, l3 = mfma(lds128(pr + 768), ones, z).x;
    const float M = vmax(vmax3(asf(m4.x), asf(m4.y), asf(m4.z)), asf(m4.w));
    const float L = fmaf(expd(asf(m4.x) - M), l0, expd(asf(m4.y) - M) * l1) + fmaf(expd(asf(m4.z) - M), l2, expd(asf(m4.w) - M) * l3);
    __builtin_amdgcn_raw_buffer_store_b128((v4u){asu(M), tag, asu(L), tag}, rs, sm_, 0, 16);
  }
  lds_wait_ge(F_D, 2);
  {
    unsigned cnt;
    asm volatile(
        "s_mov_b32 %[cnt], %[cap]\n"
        "1:\n\t"
        "s_memrealtime vcc\n\ts_waitcnt lgkmcnt(0)\n\ts_sub_u32 vcc_lo, vcc_lo, %[ref]\n\ts_cmp_lt_u32 vcc_lo, %[t]\n\ts_cbranch_scc1 1b\n"
        "2:\n\t"
        "buffer_load_dwordx4 v[100:103], %[a0], %[rs], 0 offen sc1\n\t"
        "buffer_load_dwordx4 v[104:107], %[a1], %[rs], 0 offen sc1\n\t"
        "s_sub_u32 %[cnt], %[cnt], 1\n\t"
        "s_waitcnt vmcnt(0)\n\t"
        "v_add_f32 v100, v100, v104\n\t"
        "v_add_f32 v102, v102, v106\n\t"
        "v_bitop3_b32 v101, v101, v103, %[tag] bitop3:0x7e\n\t"
        "v_bitop3_b32 v105, v105, v107, %[tag] bitop3:0x7e\n\t"
        "v_permlane32_swap_b32 v100, v102\n\t"
        "v_add_f32 v100, v100, v102\n\t"
        "v_mov_b32 v102, v100\n\t"
        "v_or_b32 v101, v101, v105\n\t"
        "v_cmp_ne_u32 vcc, 0, v101\n\t"
        "v_permlane16_swap_b32 v100, v102\n\t"
        "v_add_f32 v100, v100, v102\n\t"
        "v_cvt_pk_bf16_f32 v100, v100, v100\n\t"
        "v_lshlrev_b32 v100, 16, v100\n\t"
        "v_mul_f32 v102, 0xbfb8aa3b, v100\n\t"
        "v_exp_f32 v102, v102\n\t"
        "s_cselect_b64 vcc, 0, vcc\n\t"
        "v_add_f32 v102, 1.0, v102\n\t"
        "v_rcp_f32 v102, v102\n\t"
        "s_cbranch_vccnz 2b\n\t"
        "v_mul_f32 v100, v102, v100\n\t"
        "v_cvt_pk_bf16_f32 v100, v100, v100\n\t"
        "ds_write_b16 %[hw], v100\n\t"
        "ds_write_b32 %[fh], %[one]"
        : [cnt] "=&s"(cnt)
        : [cap] "i"(SPIN_CAP), [ref] "s"(gref), [t] "i"(T_HH), [a0] "v"(hb), [a1] "v"(hb + 4 * 1792 * 8), [rs] "s"(rs), [tag] "s"(tag),
          [hw] "v"(hw), [fh] "v"(F_H), [one] "v"(1u)
        : "memory", "scc", "vcc", "v100", "v101", "v102", "v103", "v104", "v105", "v106", "v107");
    __builtin_amdgcn_raw_buffer_store_b32(tag, rs, (b == 0 && lane == 0) ? W_EPOCH : OOB, 0, 16);
  }
  lds_wait_ge(F_D, 3);
  __builtin_amdgcn_s_sleep(D_H3);
  {
    unsigned cnt;
    asm volatile(
        "s_mov_b32 %[cnt], %[cap]\n"
        "1:\n\t"
        "s_memrealtime vcc\n\ts_waitcnt lgkmcnt(0)\n\ts_sub_u32 vcc_lo, vcc_lo, %[ref]\n\ts_cmp_lt_u32 vcc_lo, %[t]\n\ts_cbranch_scc1 1b\n"
        "2:\n\t"
        "buffer_load_dwordx4 v[100:103], %[a0], %[rs], 0 offen sc1\n\t"
        "s_sub_u32 %[cnt], %[cnt], 1\n\t"
        "s_waitcnt vmcnt(0)\n\t"
        "v_permlane32_swap_b32 v100, v102\n\t"
        "v_add_f32 v100, v100, v102\n\t"
        "v_bitop3_b32 v101, v101, v103, %[tag] bitop3:0x7e\n\t"
        "v_cmp_ne_u32 vcc, 0, v101\n\t"
        "v_add_f32_dpp v100, v100, v100 row_ror:8 row_mask:0xf bank_mask:0xf\n\t"
        "s_nop 1\n\t"
        "v_add_f32_dpp v100, v100, v100 row_ror:4 row_mask:0xf bank_mask:0xf\n\t"
        "s_cselect_b64 vcc, 0, vcc\n\t"
        "s_cbranch_vccnz 2b\n\t"
        "v_add_f32_dpp v100, v100, v100 row_ror:2 row_mask:0xf bank_mask:0xf\n\t"
        "s_nop 1\n\t"
        "v_add_f32_dpp v100, v100, v100 row_ror:1 row_mask:0xf bank_mask:0xf\n\t"
        "s_nop 1\n\t"
        "v_add_f32_dpp v100, v100, v100 row_bcast:15 row_mask:0xa bank_mask:0xf\n\t"
        "v_add_f32 v100, v100, %[xr]\n\t"
        "v_cvt_pk_bf16_f32 v100, v100, v100\n\t"
        "buffer_store_short v100, %[oo], %[ro], 0 offen"
        : [cnt] "=&s"(cnt)
        : [cap] "i"(SPIN_CAP), [ref] "s"(gref), [t] "i"(T_H3), [a0] "v"(pb), [rs] "s"(rs), [tag] "s"(tag), [xr] "v"(xr), [oo] "v"(oo), [ro] "s"(ro)
        : "memory", "scc", "vcc", "v100", "v101", "v102", "v103");
  }
}

extern "C" void kernel_launch(void* const* d_in, const int* in_sizes, int n_in, void* d_out, int out_size, void* d_ws, size_t ws_size, hipStream_t stream) {
  char* hw = (char*)d_ws + ((ws_size - W_END) & ~(size_t)4095);
  fused<<<256, 384, 0, stream>>>(hw, (const char*)d_in[0], (const char*)d_in[3], (const char*)d_in[1], (const char*)d_in[2],
                                 (const char*)d_in[6], (const char*)d_in[4], (const char*)d_in[7], (char*)d_out);
}
```
